```python
import torch

def _softmax(x, axis=-1):
    return torch.softmax(x, dim=axis)
def _gelu(x):
    return torch.nn.functional.gelu(x, approximate="tanh")
def _silu(x):
    return torch.nn.functional.silu(x)
def _sigmoid(x):
    return torch.sigmoid(x)
def _layernorm(x, axis=-1, eps=1e-5):
    m = x.mean(dim=axis, keepdim=True)
    v = x.var(dim=axis, keepdim=True, unbiased=False)
    return (x - m) / torch.sqrt(v + eps)
def _scatter_add(tgt, idx, src, axis=0):
    return tgt.clone().index_add_(axis, idx, src)
def _segment_sum(src, seg, n):
    out = torch.zeros((n,) + src.shape[1:], dtype=src.dtype)
    return out.index_add_(0, seg, src)
def _pad(x, pw):
    flat = tuple(v for pair in reversed(pw) for v in pair)
    return torch.nn.functional.pad(x, flat)

kernel_name = 'parallel_sum__mha_block__glu_gated_bilinear__prompt'

def setup_inputs():

    _w = (lambda a: (lambda t: t.to(torch.bfloat16) if t.is_floating_point() else t)(torch.as_tensor(a)))
    def _A_setup():
        B, S, D, H = 1, 2048, 640, 32
        return {
            "x": _w((lambda *s: torch.randn(*s, dtype=torch.bfloat16) * 0.05)(B, S, D)),
            "wqkv": _w((lambda *s: torch.randn(*s, dtype=torch.bfloat16) * 0.05)(D, 3 * D)),
            "wo": _w((lambda *s: torch.randn(*s, dtype=torch.bfloat16) * 0.05)(D, D)),
        }
    _A_in = _A_setup()

    def _B_setup():
        B, S, D, F = 1, 2048, 640, 1728
        return {
            "x": _w((lambda *s: torch.randn(*s, dtype=torch.bfloat16) * 0.05)(B, S, D)),
            "w_gate": _w((lambda *s: torch.randn(*s, dtype=torch.bfloat16) * 0.8)(D, F) * (D ** -0.5)),
            "w_up": _w((lambda *s: torch.randn(*s, dtype=torch.bfloat16) * 0.8)(D, F) * (D ** -0.5)),
            "w_down": _w((lambda *s: torch.randn(*s, dtype=torch.bfloat16) * 0.8)(F, D) * (F ** -0.5)),
        }
    _B_in = _B_setup()

    return {
        "A_x": _A_in["x"],
        "A_wqkv": _A_in["wqkv"],
        "A_wo": _A_in["wo"],
        "B_x": _B_in["x"],
        "B_w_gate": _B_in["w_gate"],
        "B_w_up": _B_in["w_up"],
        "B_w_down": _B_in["w_down"],
    }

def reference(A_x, A_wqkv, A_wo, B_x, B_w_gate, B_w_up, B_w_down):
    def _A_ref(x, wqkv, wo):
        B, S, D = x.shape
        H = 32
        Dh = D // H
        qkv = (lambda x, *d: x.permute(*d))((x @ wqkv).reshape(B, S, 3, H, Dh), 2, 0, 3, 1, 4)
        q, k, v = qkv[0], qkv[1], qkv[2]
        scores = torch.einsum("bhqd,bhkd->bhqk", q, k) * (Dh ** -0.5)
        p = _softmax(scores, axis=-1)
        o = torch.einsum("bhqk,bhkd->bhqd", p, v)
        return (lambda x, *d: x.permute(*d))(o, 0, 2, 1, 3).reshape(B, S, D) @ wo

    def _B_ref(x, w_gate, w_up, w_down):
        g = torch.einsum("bsd,df->bsf", x, w_gate)
        u = torch.einsum("bsd,df->bsf", x, w_up)
        h = g * u
        return torch.einsum("bsf,fd->bsd", h, w_down)

    x = A_x
    xn = _layernorm(x, axis=-1)
    return x + _A_ref(xn, A_wqkv, A_wo) + _B_ref(xn, B_w_gate, B_w_up, B_w_down)
```

```cpp
#include <hip/hip_runtime.h>
#include <hip/hip_bf16.h>
#include <stdint.h>

typedef __attribute__((ext_vector_type(16))) float f32x16;
typedef __attribute__((ext_vector_type(4))) float f32x4;
typedef __attribute__((ext_vector_type(2))) float f32x2;
typedef __attribute__((ext_vector_type(8))) __bf16 bf16x8;
typedef __attribute__((ext_vector_type(2))) __bf16 bf16x2;
typedef __attribute__((ext_vector_type(4))) short s16x4;
typedef __attribute__((ext_vector_type(4))) unsigned u32x4;
typedef __attribute__((ext_vector_type(2))) unsigned u32x2;
typedef __attribute__((address_space(3))) s16x4* lds_s16x4;

#define DEV __device__ __forceinline__
#define SB __builtin_amdgcn_sched_barrier(0)

constexpr unsigned QT_B = 1280;
constexpr unsigned VT_B = 1408;
constexpr unsigned KVT_B = QT_B + VT_B;
constexpr size_t Q_OFF = 0;
constexpr size_t KV_OFF = Q_OFF + (size_t)32 * 64 * QT_B;
constexpr size_t KN_OFF = KV_OFF + (size_t)32 * 64 * KVT_B;
constexpr size_t A_OFF = KN_OFF + 32 * 64 * 4;
constexpr unsigned AROW_B = 148 * 1024;
constexpr size_t CNT_OFF = A_OFF + (size_t)64 * AROW_B;

DEV unsigned cvt2(float a, float b) { f32x2 v = {a, b}; return __builtin_bit_cast(unsigned, __builtin_convertvector(v, bf16x2)); }
DEV float bflo(unsigned u) { return __builtin_bit_cast(float, u << 16); }
DEV float bfhi(unsigned u) { return __builtin_bit_cast(float, u & 0xffff0000u); }
DEV float dot2(unsigned a, unsigned b, float c) { return __builtin_amdgcn_fdot2_f32_bf16(__builtin_bit_cast(bf16x2, a), __builtin_bit_cast(bf16x2, b), c, false); }
DEV float xhalf(float v) {
  unsigned u = __builtin_bit_cast(unsigned, v);
  auto r = __builtin_amdgcn_permlane32_swap(u, u, false, false);
  return __builtin_bit_cast(float, (threadIdx.x & 32) ? r[0] : r[1]);
}
template <int C> DEV float dppmax(float v) {
  return fmaxf(v, __builtin_bit_cast(float, __builtin_amdgcn_update_dpp(0, __builtin_bit_cast(int, v), C, 0xf, 0xf, false)));
}
DEV float max32(float v) {
  v = dppmax<0x128>(dppmax<0x124>(dppmax<0x122>(dppmax<0x121>(v))));
  auto sw = __builtin_amdgcn_permlane16_swap(__builtin_bit_cast(unsigned, v), __builtin_bit_cast(unsigned, v), false, false);
  return fmaxf(__builtin_bit_cast(float, sw[0]), __builtin_bit_cast(float, sw[1]));
}
DEV f32x16 mfma32(u32x4 a, u32x4 b, f32x16 c) { return __builtin_amdgcn_mfma_f32_32x32x16_bf16(__builtin_bit_cast(bf16x8, a), __builtin_bit_cast(bf16x8, b), c, 0, 0, 0); }
DEV f32x16 mfma32k8(u32x2 a, u32x2 b, f32x16 c) { return __builtin_amdgcn_mfma_f32_32x32x8bf16_1k(__builtin_bit_cast(s16x4, a), __builtin_bit_cast(s16x4, b), c, 0, 0, 0); }
DEV f32x4 mfma16(u32x4 a, u32x4 b, f32x4 c) { return __builtin_amdgcn_mfma_f32_16x16x32_bf16(__builtin_bit_cast(bf16x8, a), __builtin_bit_cast(bf16x8, b), c, 0, 0, 0); }
DEV u32x2 trread(unsigned addr) { return __builtin_bit_cast(u32x2, __builtin_amdgcn_ds_read_tr16_b64_v4i16((lds_s16x4)(size_t)addr)); }
template <int V> struct IC { static constexpr int v = V; };
template <int I, int N, class Fn> DEV void sfor(Fn&& f) { if constexpr (I < N) { f(IC<I>{}); sfor<I + 1, N>(f); } }
DEV void set_m0(unsigned lds) { asm volatile("s_mov_b32 m0, %0" ::"s"(lds) : "memory"); }
template <int OFF> DEV void glds16o(unsigned voff, const void* sbase) {
  asm volatile("global_load_lds_dwordx4 %0, %1 offset:%2" ::"v"(voff), "s"(sbase), "i"(OFF) : "memory");
}
template <int OFF> DEV void ldfrag(u32x4& a, unsigned voff, const void* b) { asm volatile("global_load_dwordx4 %0, %1, %2 offset:%3" : "=v"(a) : "v"(voff), "s"(b), "i"(OFF) : "memory"); }
DEV void pin2(u32x4& a, u32x4& b) { asm volatile("" : "+v"(a), "+v"(b)); }
constexpr int K2_CODE = 20480;
DEV const char* k2code() {
  const char* p; int o;
  asm volatile("s_getpc_b64 %0\n.Lk2pc%=:\n\ts_mov_b32 %1, _Z7k2_mainPKcS0_S0_PcS1_-.Lk2pc%=" : "=s"(p), "=s"(o));
  return p + o;
}
DEV const char* getpc() { const char* p; asm volatile("s_getpc_b64 %0" : "=s"(p)); return p; }
DEV void st16(const void* sb, unsigned vo, u32x4 v) { asm volatile("global_store_dwordx4 %0, %1, %2 sc1\n\ts_nop 1" ::"v"(vo), "v"(v), "s"(sb) : "memory"); }
DEV void st8(const void* sb, unsigned vo, u32x2 v) { asm volatile("global_store_dwordx2 %0, %1, %2 sc1\n\ts_nop 1" ::"v"(vo), "v"(v), "s"(sb) : "memory"); }
#define WAIT_VM(n) asm volatile("s_waitcnt vmcnt(%0)" ::"i"(n) : "memory")
#define BAR() asm volatile("s_barrier" ::: "memory")
#define WAIT_LGKM0() asm volatile("s_waitcnt lgkmcnt(0)" ::: "memory")

constexpr unsigned K1_STG = 2 * 64 * 352 + 256;
constexpr int K1_LDS = 2 * K1_STG + 4 * 16384;
DEV float fma1(float a, float b, float c) { float d; asm("v_fma_f32 %0, %1, %2, %3" : "=v"(d) : "v"(a), "v"(b), "v"(c)); return d; }
template <int NB> DEV constexpr int posb(int p) { const int m = p - 36 * NB; return m < 0 ? p % NB : m < 8 * (NB / 2) ? 2 * (m / 8) + (m & 1) : NB - 1; }
template <int NB> DEV constexpr int posg(int p) { const int m = p - 36 * NB; return m < 0 ? p / NB : m < 8 * (NB / 2) ? 36 + (m % 8) / 2 : 36 + m - 8 * (NB / 2); }
template <int MODE, int NB>
DEV void k1_tile(const char* __restrict__ x, const char* P0, const char* P1, const unsigned ldw2, const int rb, const int c0, char* __restrict__ ws, const char* wd, const char* wo) {
  constexpr unsigned RS = NB * 32, R1 = 64 * RS + (NB == 11 ? 128 : 32), STG = K1_STG;
  const int lane = threadIdx.x & 63, w = __builtin_amdgcn_readfirstlane(threadIdx.x >> 6);
  const int r = lane & 31, h = lane >> 5;
  const int tile = rb * 4 + w;
  unsigned goff[NB];
  const char* wp[10];
  const unsigned ldst = (w >> 1) * R1 + (w & 1) * 32 * RS;
  auto stm0 = [&](int t, int i) { if ((i & 3) == 0) set_m0((t % 3) * STG + ldst + i * 1024); };
  auto stld = [&](int t, int i) {
    const char* b = wp[t];
    if ((i & 3) == 0) glds16o<0>(goff[i], b); else if ((i & 3) == 1) glds16o<1024>(goff[i], b);
    else if ((i & 3) == 2) glds16o<2048>(goff[i], b); else glds16o<3072>(goff[i], b);
  };
  u32x4 xr[40];
  f32x16 gr = {};
  f32x4 sm = {};
  const unsigned on = ((lane >> 2) ^ (lane >> 4)) & 1 ? 0u : 0x3f803f80u;
  const u32x4 sel = {on, on, on, on};
  {
    typedef __attribute__((address_space(3))) u32x4* lp;
    const char* xb = x + (size_t)tile * 32 * 1280;
    const unsigned sa = 2 * STG + w * 16384, sb = STG + ((ldst + 127) & ~127u) - 4 * 4096;
    constexpr int PW = 3;
    unsigned xo[4], ra[4];
#pragma unroll
    for (int j = 0; j < 4; j++) {
      xo[j] = (8 * j + (lane >> 3)) * 1280 + (((lane & 7) ^ (lane >> 4) ^ (4 * (j & 1))) << 4) - j * 1024;
      ra[j] = r * 128 + (((2 * j + h) ^ ((r >> 1) & 7)) << 4);
    }
    auto xld = [&](int c) {
      set_m0((c % 6 < 4 ? sa : sb) + (c % 6) * 4096); asm volatile("s_nop 0");
      glds16o<0>(xo[0], xb + c * 128); glds16o<1024>(xo[1], xb + c * 128); glds16o<2048>(xo[2], xb + c * 128); glds16o<3072>(xo[3], xb + c * 128);
    };
    u32x4 pf0, pf1;
    { const char* pc = getpc() + (tile & 15) * 2048; ldfrag<0>(pf0, lane * 16, pc); ldfrag<1024>(pf1, lane * 16, pc); }
#pragma unroll
    for (int c = 0; c < 6; c++) xld(c);
    int ln = lane, w1 = w;
    asm volatile("" : "+v"(ln), "+s"(w1));
#pragma unroll
    for (int i = 0; i < NB; i++) {
      const int e = 64 * i + (ln & 63), row = e / (2 * NB);
      goff[i] = (unsigned)(row + 32 * (w & 1)) * ldw2 + (e - row * 2 * NB) * 16 - (i & 3) * 1024;
      asm volatile("" : "+v"(goff[i]));
    }
#pragma unroll
    for (int t = 0; t < 10; t++) { wp[t] = ((w1 & 2) ? P1 : P0) + (size_t)(t * 64 * ldw2); asm volatile("" : "+s"(wp[t])); }
    sfor<0, 11>([&](auto C) __attribute__((always_inline)) {
      constexpr int c = C.v;
      if constexpr (c < 10) {
        WAIT_VM((c < 4 ? 24 + 4 * c : 40 + (PW * (c - 3) < NB ? PW * (c - 3) : NB)) - 4 * (c + 1));
#pragma unroll
        for (int j = 0; j < 4; j++) xr[4 * c + j] = *(lp)(size_t)(ra[j] + (c % 6 < 4 ? sa : sb) + (c % 6) * 4096);
      }
      if (c == 0) pin2(pf0, pf1);
      SB;
      if constexpr (c > 0) {
#pragma unroll
        for (int i = 4 * c - 4; i < 4 * c; i++) { gr = mfma32(xr[i], xr[i], gr); sm = mfma16(sel, xr[i], sm); }
      }
      SB;
      if (c < 4) { WAIT_LGKM0(); xld(c + 6); }
#pragma unroll
      for (int i = PW * (c - 3); i < PW * (c - 2); i++)
        if (i >= 0 && i < NB) { stm0(0, i); if ((i & 3) == 0) asm volatile("s_nop 0"); stld(0, i); }
      SB;
    });
  }
  const unsigned rbase = ((lane >> 4) & 1) * R1 + (8 * h + ((lane & 15) >> 2)) * RS + (lane & 3) * 8;
  unsigned rb3[3] = {rbase, rbase + STG, rbase + 2 * STG};
  asm volatile("" : "+v"(rb3[1]), "+v"(rb3[2]));
  auto rdw = [&](int g, int nb) {
    const unsigned a = rb3[(g >> 2) % 3];
    const u32x2 lo = trread(a + 16 * (g & 3) * RS + nb * 32), hi = trread(a + (16 * (g & 3) + 4) * RS + nb * 32);
    return (u32x4){lo[0], lo[1], hi[0], hi[1]};
  };
  float q0 = 0.f;
#pragma unroll
  for (int i = 0; i < 16; i++) q0 = (r == (i & 3) + 8 * (i >> 2) + 4 * h) ? gr[i] : q0;
  q0 += xhalf(q0);
  const float mean = sm[0] * (1.f / 640.f);
  const float var = fmaxf(q0 * (1.f / 640.f) - mean * mean, 0.f);
  const float rstd = __builtin_amdgcn_rsqf(var + 1e-5f) * (MODE == 0 ? 0.22360679774997896f * 1.4426950408889634f : 1.f), nm = -mean * rstd;
#pragma unroll
  for (int i = 0; i < 4; i++) {
#pragma unroll
    for (int j = 0; j < 4; j++) xr[i][j] = cvt2(fma1(bflo(xr[i][j]), rstd, nm), fma1(bfhi(xr[i][j]), rstd, nm));
    asm volatile("" : "+v"(xr[i]));
  }
  u32x4 wf[NB];
  WAIT_VM(0);
  BAR();
#pragma unroll
  for (int nb = 0; nb < NB - 1; nb++) wf[nb] = rdw(0, nb);
  f32x16 acc[NB];
  typedef __attribute__((address_space(3))) u32x4* lp;
  constexpr unsigned HS = 64 * (MODE == 0 ? QT_B : KVT_B);
  char* const ob = MODE == 3 ? ws + A_OFF + (size_t)tile * AROW_B + c0 * 1024
                             : ws + (MODE == 0 ? Q_OFF : KV_OFF) + (size_t)tile * (HS / 64) + (size_t)(c0 / 20) * HS + (MODE == 2 ? QT_B : 0);
  const unsigned vb = STG + w * 16 * VT_B;
  unsigned pv[NB][8], va[16];
  u32x4 vv[11];
  auto conv = [&](int k) {
    const int b = k >> 2, j = k & 3;
    if (MODE == 3) {
      pv[b][j] = cvt2(acc[b][2 * j] * acc[b][8 + 2 * j], acc[b][2 * j + 1] * acc[b][9 + 2 * j]);
      asm volatile("" : "+v"(pv[b][j]));
      if (j == 3) st16(ob + b * 1024, lane * 16, (u32x4){pv[b][0], pv[b][1], pv[b][2], pv[b][3]});
    } else {
      pv[b][2 * j] = cvt2(acc[b][4 * j], acc[b][4 * j + 1]); pv[b][2 * j + 1] = cvt2(acc[b][4 * j + 2], acc[b][4 * j + 3]);
      asm volatile("" : "+v"(pv[b][2 * j]), "+v"(pv[b][2 * j + 1]));
      if (MODE == 2 && (j & 1)) *(lp)(size_t)(va[11 + b % 5] + (b / 5) * 8 * VT_B + (j >> 1) * 672) = (u32x4){pv[b][2 * j - 2], pv[b][2 * j - 1], pv[b][2 * j], pv[b][2 * j + 1]};
    }
  };
  auto qv = [&](int a, int m, int i) { const int j = 5 * m + (i >> 1); return pv[j >> 1][4 * a + 2 * (j & 1) + (i & 1)]; };
  auto qst = [&](int n) {
    const int m = n / 6, a = (n % 6) / 3, kind = n % 3, i = 3 * kind;
    char* hb = ob + (size_t)(8 * a + 2 * m) * HS;
    if (kind == 1) st8(hb, h * HS + 1024 + r * 8, (u32x2){qv(a, m, 4), qv(a, m, 5)});
    else st16(hb + (kind >> 1) * HS, lane * 16, (u32x4){qv(a, m, i), qv(a, m, i + 1), qv(a, m, i + 2), qv(a, m, i + 3)});
  };
  auto vad = [&](int i) {
    if (i < 11) { const int e = 64 * i + lane, sl = e / 88; va[i] = (sl + (sl & 4)) * HS + (e - 88 * sl) * 16; }
    else { const int c = 16 * (i - 11) + (r & 15), hd = c / 20; va[i] = vb + (hd + 4 * (r >> 4)) * VT_B + (c - 20 * hd) * 16 + h * 336; }
    asm volatile("" : "+v"(va[i]));
  };
  auto vrd = [&](int u, int i) { vv[i] = *(lp)(size_t)(vb + lane * 16 + i * 1024 + u * 8 * VT_B); };
  auto vst = [&](int u, int i) { st16(ob + u * 4 * HS, va[i], vv[i]); };
  SB;
  sfor<0, 40>([&](auto G) __attribute__((always_inline)) {
    constexpr int g = G.v;
    u32x4 xnn;
    float lo[4], hi[4];
    const int t = g >> 2;
    const bool edge = (g & 3) == 3 && g < 39;
#pragma unroll
    for (int nb = 0; nb < NB; nb++) {
      const f32x16 z = {};
      const int p0 = g * NB + nb, pos = p0 - (3 * NB + 3), pt = pos / (4 * NB), pp = pos - pt * 4 * NB;
      const int si = p0 < NB ? p0 : pos < 0 ? (p0 - NB) / 2 : pp / 4;
      const int st = p0 < NB ? 1 : pos < 0 ? ((p0 - NB) % 2 == 0 && si < NB ? 2 : -1) : (pp % 4 == 0 && pt + 3 < 10) ? pt + 3 : -1;
      const int b = posb<NB>(p0), gs = posg<NB>(p0), k = p0 - 36 * NB - 8;
      acc[b] = (MODE == 2) ? mfma32(xr[gs], wf[nb], g ? acc[b] : z) : mfma32(wf[nb], xr[gs], g ? acc[b] : z);
      SB;
      if (st >= 0) stm0(st, si);
      if (g < 36) {
        const u32x4 u = xr[g < 36 ? g + 4 : g];
#define FL(j) lo[j] = fma1(lo[j], rstd, nm)
#define CV(j) { unsigned c_ = cvt2(lo[j], hi[j]); asm volatile("" : "+v"(c_)); xnn[j] = c_; }
#define FH(j) hi[j] = fma1(hi[j], rstd, nm)
        if (nb == 0) { lo[0] = bflo(u[0]); hi[0] = bfhi(u[0]); }
        if (nb == 1) { FL(0); FH(0); lo[1] = bflo(u[1]); }
        if (nb == 2) { CV(0); hi[1] = bfhi(u[1]); }
        if (nb == 3) { FL(1); FH(1); }
        if (nb == 4) { CV(1); lo[2] = bflo(u[2]); }
        if (nb == 5) { FL(2); hi[2] = bfhi(u[2]); }
        if (nb == 6) { FH(2); lo[3] = bflo(u[3]); }
        if (nb == 7) { CV(2); hi[3] = bfhi(u[3]); }
        if (nb == 8) { FL(3); FH(3); }
        if (nb == 9) CV(3);
        SB;
      }
      const int pn = p0 + NB - 1;
      if (nb == 0) wf[NB - 1] = rdw(posg<NB>(pn), posb<NB>(pn));
      else if (g < 39) {
        if (!edge || nb > 2) wf[nb - 1] = rdw(posg<NB>(pn), posb<NB>(pn));
        else if (nb == 2) {
          WAIT_LGKM0();
          if (t + 2 < 10) WAIT_VM(NB); else WAIT_VM(4);
          BAR();
          wf[0] = rdw(posg<NB>(pn - 1), posb<NB>(pn - 1)); wf[1] = rdw(posg<NB>(pn), posb<NB>(pn));
        }
      }
      if (MODE == 2 && k >= -40 && k < -8 && (k & 1)) vad((k + 40) >> 1);
      if (g >= 36) {
        if (MODE < 2) { if (k >= 12 && k < 18) qst(k - 12); if (k >= 20 && k < 26) qst(k - 14); }
        if (MODE == 2) {
          if (k == -2) *(lp)(size_t)(vb + (r >> 1) * VT_B + (r & 1) * 672 + 320 + h * 336) = (u32x4){0x3f803f80u, 0x3f803f80u, 0x3f803f80u, 0x3f803f80u};
          if (k == 0) *(lp)(size_t)(vb + (lane >> 2) * VT_B + 1344 + (lane & 3) * 16) = (u32x4){0u, 0u, 0u, 0u};
          if (k >= 23) vst(0, k - 23);
          if (k >= 20 && k < 31) vrd(0, k - 20);
        }
        if (k >= 0) conv(k);
      }
      asm volatile("" ::"v"(wf[nb]), "v"(xr[gs]));
      SB;
      if (st >= 0) { stld(st, si); SB; }
      if (g >= 32 && g < 35 && nb == 4) {
        const int j = blockIdx.x * 4 + w + 1024 * (g - 32);
        set_m0(3 * STG + w * 1024); asm volatile("s_nop 0");
        glds16o<0>(lane * 16, j < 2160 ? wd + j * 1024 : wo + min(j - 2160, 799) * 1024); SB;
        if (g == 34) { glds16o<0>(lane * 16, k2code() + ((blockIdx.x >> 3) * 4 + w) % (K2_CODE / 1024) * 1024); SB; }
      }
    }
    if (g < 36) xr[g < 36 ? g + 4 : g] = xnn;
  });
  if (MODE < 2) {
#pragma unroll
    for (int n = 12; n < 18; n++) qst(n);
  }
  if (MODE == 2) { vst(0, 9); vst(0, 10); }
#pragma unroll
  for (int k = 4 * NB - 8; k < 4 * NB; k++) conv(k);
  if (MODE < 2) {
#pragma unroll
    for (int n = 18; n < 24; n++) qst(n);
  }
  if (MODE == 2) {
#pragma unroll
    for (int i = 0; i < 11; i++) vrd(1, i);
#pragma unroll
    for (int i = 0; i < 11; i++) vst(1, i);
  }
  if (MODE == 1) {
    float kmx = 0.f;
#pragma unroll
    for (int n = 0; n < 8; n++) {
      const int a = n >> 2, m = n & 3;
#define QD(i, c) dot2(qv(a, m, i), qv(a, m, i), c)
      const float tt = QD(4, QD(5, 0.f));
      const float na = QD(0, QD(1, QD(2, QD(3, h ? 0.f : tt)))), nbb = QD(6, QD(7, QD(8, QD(9, h ? tt : 0.f))));
      auto sw = __builtin_amdgcn_permlane32_swap(__builtin_bit_cast(unsigned, na), __builtin_bit_cast(unsigned, nbb), false, false);
      const float k2 = __builtin_bit_cast(float, sw[0]) + __builtin_bit_cast(float, sw[1]), km = max32(k2 < 3.0e38f ? k2 : 3.0e38f);
      kmx = r == n ? km : kmx;
    }
    if (r < 8) ((float*)(ws + KN_OFF))[(c0 / 20 + 2 * r + h) * 64 + tile] = kmx;
  }
  WAIT_VM(0);
}

__global__ __launch_bounds__(256, 1) void k1_proj(const char* __restrict__ x, const char* __restrict__ wqkv, const char* __restrict__ wg,
                                                  const char* __restrict__ wu, char* __restrict__ ws, const char* wd, const char* wo) {
  const int xcd = blockIdx.x & 7, i = blockIdx.x >> 3;
  const int rb = (xcd & 3) * 4 + (i & 3), nt = (xcd >> 2) + 2 * (i >> 2);
  if (blockIdx.x == 0 && threadIdx.x < 32) *(unsigned*)(ws + CNT_OFF + threadIdx.x * 256) = 0u;
  if (nt < 6) {
    const char* p = wqkv + nt * 640;
    const int c0 = (nt & 1) * 320;
    if (nt < 2) k1_tile<0, 10>(x, p, p + 320, 3840, rb, c0, ws, wd, wo);
    else if (nt < 4) k1_tile<1, 10>(x, p, p + 320, 3840, rb, c0, ws, wd, wo);
    else k1_tile<2, 10>(x, p, p + 320, 3840, rb, c0, ws, wd, wo);
  } else {
    const int b0 = min(11 * (nt - 6), 97);
    k1_tile<3, 11>(x, wg + b0 * 32, wu + b0 * 32, 3456, rb, b0, ws, wd, wo);
  }
}

constexpr unsigned G_REG = 30720, K2_KV = 4 * G_REG, K2_CMB = K2_KV + 12288;
constexpr int K2_LDS = K2_CMB + 4 * 2 * 13 * 256 + 1024;
typedef __attribute__((address_space(3))) float* lds_f32;
typedef __attribute__((address_space(3))) f32x4* lds_f32x4;
template <int OFF> DEV void ldfrag1(u32x4& a, unsigned voff, const void* b) { asm volatile("global_load_dwordx4 %0, %1, %2 offset:%3 sc1" : "=v"(a) : "v"(voff), "s"(b), "i"(OFF) : "memory"); }
template <int OFF> DEV void ldtail1(u32x2& a, unsigned voff, const void* b) { asm volatile("global_load_dwordx2 %0, %1, %2 offset:%3 sc1" : "=v"(a) : "v"(voff), "s"(b), "i"(OFF) : "memory"); }

DEV void k2_kv(const char* kvb, const int gw, const int c) {
  const unsigned vo = (threadIdx.x & 63) * 16;
  const char* b = kvb + (size_t)c * 2 * KVT_B;
  set_m0(K2_KV + (c % 3) * 12288 + gw * 3072); asm volatile("s_nop 0");
  glds16o<0>(vo, b); glds16o<1024>(vo, b); glds16o<2048>(vo, b);
}
template <class Fx> DEV void k2_gemm(const char* __restrict__ wd, const char* __restrict__ wo, char* __restrict__ ws, const char* kvb, const int gw, const int mt, const int cb, const int qbc, Fx&& ldx) {
  const int lane = threadIdx.x & 63, h = lane >> 5;
  unsigned goff[3], gofft[3];
#pragma unroll
  for (int i = 0; i < 3; i++) {
    const int e = 64 * i + lane, kl = e / 12, pc = (e - 12 * kl) * 16 + cb - i * 1024;
    goff[i] = kl * 1280 + pc;
    gofft[i] = (20 * (kl >> 2) + 16 + (kl & 3)) * 1280 + pc;
  }
  const unsigned lw = gw * G_REG, aoff = lane * 16;
  const unsigned rbase = lw + (4 * h + ((lane & 15) >> 2)) * 192 + ((lane >> 4) & 1) * 32 + (lane & 3) * 8;
  const char* ab = ws + A_OFF + (size_t)mt * 2 * AROW_B + gw * 27 * 1024;
  const char* wb = wd + (size_t)gw * 27 * 16 * 1280;
  auto wstep = [&](unsigned l, const char* b, const unsigned* go) {
    set_m0(l); asm volatile("s_nop 0");
    glds16o<0>(go[0], b); glds16o<1024>(go[1], b); glds16o<2048>(go[2], b);
  };
  u32x4 af[16];
  auto stage = [&](int n, auto SL) {
    constexpr int sl = SL.v;
    wstep(lw + sl * 3072, wb + (size_t)n * 16 * 1280, goff);
    ldfrag<0>(af[2 * sl], aoff, ab + n * 1024); ldfrag<0>(af[2 * sl + 1], aoff, ab + n * 1024 + AROW_B);
  };
  f32x16 acc[6];
#pragma unroll
  for (int i = 0; i < 6; i++) acc[i] = (f32x16){};
  auto step = [&](unsigned off, const u32x4& b0, const u32x4& b1) {
#pragma unroll
    for (int fb = 0; fb < 3; fb++) {
      const u32x2 lo = trread(rbase + off + fb * 64), hi = trread(rbase + off + fb * 64 + 8 * 192);
      const u32x4 wf = {lo[0], lo[1], hi[0], hi[1]};
      acc[2 * fb] = mfma32(wf, b0, acc[2 * fb]); acc[2 * fb + 1] = mfma32(wf, b1, acc[2 * fb + 1]);
    }
  };
  auto wost = [&](int st) { wstep(lw + st * 3072, wo + (size_t)(gw * 160 + (st < 8 ? st * 20 : (st - 8) * 80)) * 1280, st < 8 ? goff : gofft); };
  stage(0, IC<0>{}); stage(1, IC<1>{});
  WAIT_VM(13);
  sfor<0, 16>([&](auto IT) __attribute__((always_inline)) {
    constexpr int it = IT.v;
    BAR();
    if (it < 14) k2_kv(kvb, gw, it + 2);
    sfor<2 * it, (2 * it + 2 < 27 ? 2 * it + 2 : 27)>([&](auto N) __attribute__((always_inline)) {
      constexpr int n = N.v, sl = n % 3;
      constexpr int issued = 16 + 3 * (n / 2 + 1) + 5 * ((n + 2 < 26 ? n + 2 : 26) - 1);
      constexpr int need = n == 0 ? 11 : n == 1 ? 16 : 16 + 3 * ((n - 2) / 2 + 1) + 5 * (n - 1);
      if constexpr (n + 2 < 27) stage(n + 2, IC<(n + 2) % 3>{});
      asm volatile("s_waitcnt vmcnt(%2)" : "+v"(af[2 * sl]), "+v"(af[2 * sl + 1]) : "i"(issued - need) : "memory");
      step(sl * 3072, af[2 * sl], af[2 * sl + 1]);
      asm volatile("" : "+v"(acc[0]), "+v"(acc[1]), "+v"(acc[2]), "+v"(acc[3]), "+v"(acc[4]), "+v"(acc[5])::"memory");
      SB;
    });
    if (it == 13) { wost(0); wost(1); wost(2); wost(3); }
    if (it == 14) { wost(4); wost(5); wost(6); WAIT_VM(21); }
    if (it == 15) { wost(7); wost(8); wost(9); }
  });
  ldx();
  BAR();
  BAR();
  {
    const char* cp = ws + CNT_OFF + (qbc * 4 + gw) * 256;
    unsigned c;
    do {
      asm volatile("s_sleep 1\n\tglobal_load_dword %0, %1, %2 sc1\n\ts_waitcnt vmcnt(0)" : "=v"(c) : "v"(0u), "s"(cp) : "memory");
    } while (__builtin_amdgcn_readfirstlane(c) < 8u);
  }
  {
    const char* a0 = ws + A_OFF + (size_t)mt * 2 * AROW_B + (108 + gw * 10) * 1024;
    sfor<0, 8>([&](auto ST) __attribute__((always_inline)) {
      constexpr int st = ST.v;
      const char* b = a0 + (st >> 2) * 4096;
      ldfrag1<(st & 3) * 1024>(af[2 * st], aoff, b); ldfrag1<(st & 3) * 1024>(af[2 * st + 1], aoff, b + AROW_B);
    });
    u32x2 tl[8];
    sfor<0, 8>([&](auto I) __attribute__((always_inline)) {
      constexpr int i = I.v;
      ldtail1<(i >> 2) * 1024 + (i & 1) * 512>(tl[i], aoff >> 1, a0 + 8192 + ((i >> 1) & 1) * AROW_B);
    });
    sfor<0, 8>([&](auto ST) __attribute__((always_inline)) {
      constexpr int st = ST.v;
      asm volatile("s_waitcnt vmcnt(%2)" : "+v"(af[2 * st]), "+v"(af[2 * st + 1]) : "i"(22 - 2 * st) : "memory");
      step(st * 3072, af[2 * st], af[2 * st + 1]);
    });
    asm volatile("s_waitcnt vmcnt(0)" : "+v"(tl[0]), "+v"(tl[1]), "+v"(tl[2]), "+v"(tl[3]), "+v"(tl[4]), "+v"(tl[5]), "+v"(tl[6]), "+v"(tl[7])::"memory");
#pragma unroll
    for (int i = 0; i < 8; i += 4)
      step((8 + i / 4) * 3072, (u32x4){tl[i][0], tl[i][1], tl[i + 1][0], tl[i + 1][1]}, (u32x4){tl[i + 2][0], tl[i + 2][1], tl[i + 3][0], tl[i + 3][1]});
  }
  WAIT_LGKM0();
#pragma unroll
  for (int i = 0; i < 6; i++)
#pragma unroll
    for (int q = 0; q < 4; q++)
      *(lds_f32x4)(size_t)(lw + ((i & 1) * 32 + (lane & 31)) * 400 + ((i >> 1) * 32 + 8 * q + 4 * h) * 4) = (f32x4){acc[i][4 * q], acc[i][4 * q + 1], acc[i][4 * q + 2], acc[i][4 * q + 3]};
}

DEV void k2_attn(char* __restrict__ ws, const int w, const int head, const int qb) {
  constexpr int NT = 32;
  const int lane = threadIdx.x & 63;
  const int r = lane & 31, h = lane >> 5;
  const int qt0 = qb * 8 + (w & 1) * 4, kt0 = (w >> 1) * NT;
  const char* kv = ws + KV_OFF + (size_t)(head * 64 + kt0) * KVT_B;
  const unsigned ok16 = lane * 16, ok4 = 1024 + r * 8, ov = QT_B + h * 336 + min(r, 20) * 16;
  u32x4 q16[4]; u32x2 q4[4];
  float qn = 0.f;
  u32x4 k16 = *(const u32x4*)(kv + ok16), v0 = *(const u32x4*)(kv + ov), v1 = *(const u32x4*)(kv + ov + 672);
  u32x2 k4 = *(const u32x2*)(kv + ok4);
#pragma unroll
  for (int j = 0; j < 4; j++) {
    const char* qp = ws + Q_OFF + (size_t)(head * 64 + qt0 + j) * QT_B;
    q16[j] = *(const u32x4*)(qp + ok16);
    q4[j] = *(const u32x2*)(qp + ok4);
    if (h) q4[j] = (u32x2){0u, 0u};
  }
  const float kn = max32(((const float*)(ws + KN_OFF))[head * 64 + kt0 + r]);
#pragma unroll
  for (int j = 0; j < 4; j++) {
    float s = dot2(q4[j][0], q4[j][0], dot2(q4[j][1], q4[j][1], 0.f));
#pragma unroll
    for (int e = 0; e < 4; e++) s = dot2(q16[j][e], q16[j][e], s);
    qn = fmaxf(qn, s + xhalf(s));
  }
  asm volatile("" : "+v"(k16), "+v"(k4), "+v"(v0), "+v"(v1));
  const bool fast = __all(qn * kn <= 6400.f);
  f32x16 o[4];
  float mx[4];
#pragma unroll
  for (int j = 0; j < 4; j++) mx[j] = 0.f;
  const f32x16 z = {};
  if (fast) {
#define EXG(Sv, b, P, i)                                                                                                   \
    { const float e0 = __builtin_amdgcn_exp2f(Sv[b]), e1 = __builtin_amdgcn_exp2f(Sv[b + 1]), e2 = __builtin_amdgcn_exp2f(Sv[b + 2]),        \
                  e3 = __builtin_amdgcn_exp2f(Sv[b + 3]); SB;                                                              \
      unsigned c0_ = cvt2(e0, e1), c1_ = cvt2(e2, e3); asm volatile("" : "+v"(c0_), "+v"(c1_)); P[i] = c0_; P[i + 1] = c1_; SB; }
#define TILE(SC, SN, KN16, KN4, JN, OP, VP, OC, VC, X0, X1, X2, X3)            \
    SN = mfma32(KN16, q16[JN], z); SB; X0 EXG(SC, 0, p0, 0)                    \
    OP = mfma32(VP, p1, OP); SB; X1 EXG(SC, 4, p0, 2)                          \
    SN = mfma32k8(KN4, q4[JN], SN); SB; X2 EXG(SC, 8, p1, 0)                   \
    OC = mfma32(VC, p0, OC); SB; X3 EXG(SC, 12, p1, 2)
#define LD4(R, a, off) asm volatile("ds_read_b128 %0, %1 offset:%2" : "=v"(R) : "v"(a), "i"(off) : "memory"); SB;
#define LD2(R, a, off) asm volatile("ds_read_b64 %0, %1 offset:%2" : "=v"(R) : "v"(a), "i"(off) : "memory"); SB;
#define STEP(K16, K4, V0, V1, N16, N4, NV0, NV1, off, X)                                                                \
    TILE(sa, sb, K16, K4, 1, o[3], NV1, o[0], V0, , , , X)                                                                 \
    TILE(sb, sa, K16, K4, 2, o[0], V1, o[1], V0, LD4(N16, la16, off), LD2(N4, la4, off), LD4(NV0, lav, off), LD4(NV1, lav, off + 672)) \
    TILE(sa, sb, K16, K4, 3, o[1], V1, o[2], V0, , , , asm volatile("s_waitcnt lgkmcnt(0)" : "+v"(N16), "+v"(N4), "+v"(NV0), "+v"(NV1)::"memory"); SB;) \
    TILE(sb, sa, N16, N4, 0, o[2], V1, o[3], V0, , , , )
    f32x16 sa = mfma32k8(k4, q4[0], mfma32(k16, q16[0], z)), sb;
    u32x4 p0, p1 = {0u, 0u, 0u, 0u};
    u32x4 nk16, nv0, nv1 = v1; u32x2 nk4;
    const unsigned lb = K2_KV + (w >> 1) * 6144;
    const unsigned la16 = lb + ok16, la4 = lb + ok4, lav = lb + ov;
    SB;
#pragma unroll
    for (int j = 0; j < 4; j++) o[j] = (f32x16){};
    asm volatile("" : "+v"(o[0]), "+v"(o[1]), "+v"(o[2]), "+v"(o[3]));
    SB;
#define ITER(so) STEP(k16, k4, v0, v1, nk16, nk4, nv0, nv1, so, BAR(); SB;) STEP(nk16, nk4, nv0, nv1, k16, k4, v0, v1, so + KVT_B, )
    ITER(0)
    for (int t = 0; t < 5; t++) { ITER(12288) ITER(24576) ITER(0) }
    o[3] = mfma32(nv1, p1, o[3]);
  } else {
#pragma unroll
    for (int j = 0; j < 4; j++) { o[j] = (f32x16){}; mx[j] = -3.0e38f; }
    for (int t = 0; t < NT; t++) {
      const char* kt = kv + (size_t)t * KVT_B;
      if (t & 1) BAR();
      k16 = *(const u32x4*)(kt + ok16); k4 = *(const u32x2*)(kt + ok4);
      v0 = *(const u32x4*)(kt + ov); v1 = *(const u32x4*)(kt + ov + 672);
#pragma unroll
      for (int j = 0; j < 4; j++) {
        f32x16 s = mfma32k8(k4, q4[j], mfma32(k16, q16[j], z));
        float m = mx[j];
#pragma unroll
        for (int e = 0; e < 16; e++) m = fmaxf(m, s[e]);
        m = fmaxf(m, xhalf(m));
        const float al = __builtin_amdgcn_exp2f(mx[j] - m);
        mx[j] = m;
        u32x4 p0, p1;
#pragma unroll
        for (int e = 0; e < 4; e++) {
          p0[e] = cvt2(__builtin_amdgcn_exp2f(s[2 * e] - m), __builtin_amdgcn_exp2f(s[2 * e + 1] - m));
          p1[e] = cvt2(__builtin_amdgcn_exp2f(s[8 + 2 * e] - m), __builtin_amdgcn_exp2f(s[9 + 2 * e] - m));
        }
        o[j] = mfma32(v1, p1, mfma32(v0, p0, o[j] * al));
      }
    }
  }
  const int kh = w >> 1;
  if (kh) {
    f32x16 t0 = o[0], t1 = o[1]; o[0] = o[2]; o[1] = o[3]; o[2] = t0; o[3] = t1;
    float u0 = mx[0], u1 = mx[1]; mx[0] = mx[2]; mx[1] = mx[3]; mx[2] = u0; mx[3] = u1;
  }
#pragma unroll
  for (int j = 0; j < 2; j++) {
    const unsigned p = K2_CMB + (w * 2 + j) * 3328;
#pragma unroll
    for (int e = 0; e < 3; e++) *(lds_f32x4)(size_t)(p + e * 1024 + lane * 16) = (f32x4){o[2 + j][4 * e], o[2 + j][4 * e + 1], o[2 + j][4 * e + 2], o[2 + j][4 * e + 3]};
    *(lds_f32)(size_t)(p + 3072 + lane * 4) = mx[2 + j];
  }
  WAIT_LGKM0();
  BAR();
#pragma unroll
  for (int j = 0; j < 2; j++) {
    const unsigned p = K2_CMB + ((w ^ 2) * 2 + j) * 3328;
    const float m2 = *(lds_f32)(size_t)(p + 3072 + lane * 4), m = fmaxf(m2, mx[j]);
    const float f1 = __builtin_amdgcn_exp2f(mx[j] - m), f2 = __builtin_amdgcn_exp2f(m2 - m);
    float a[12];
#pragma unroll
    for (int e = 0; e < 3; e++) {
      const f32x4 v = *(lds_f32x4)(size_t)(p + e * 1024 + lane * 16);
#pragma unroll
      for (int k = 0; k < 4; k++) a[4 * e + k] = fmaf(v[k], f2, o[j][4 * e + k] * f1);
    }
    const float ox = xhalf(a[8]);
    const float inv = 1.f / (h ? a[8] : ox);
    const char* base = ws + A_OFF + (size_t)(qt0 + 2 * kh + j) * AROW_B + (108 + (head >> 3) * 10) * 1024;
    const u32x4 v = {cvt2(a[0] * inv, a[1] * inv), cvt2(a[2] * inv, a[3] * inv), cvt2(a[4] * inv, a[5] * inv), cvt2(a[6] * inv, a[7] * inv)};
    const u32x2 t = {cvt2(a[8] * inv, a[9] * inv), cvt2(a[10] * inv, a[11] * inv)};
    asm volatile("global_store_dwordx4 %0, %1, %2 sc1\n\ts_nop 1" ::"v"((head & 7) * 1024 + lane * 16), "v"(v), "s"(base) : "memory");
    if (h == 0)
      asm volatile("global_store_dwordx2 %0, %1, %2 sc1\n\ts_nop 1" ::"v"(8192 + (head & 7) * 256 + r * 8), "v"(t), "s"(base) : "memory");
  }
  WAIT_VM(0);
  BAR();
  if (threadIdx.x == 0) __hip_atomic_fetch_add((unsigned*)(ws + CNT_OFF + (qb * 4 + (head >> 3)) * 256), 1u, __ATOMIC_RELAXED, __HIP_MEMORY_SCOPE_AGENT);
}

__global__ __launch_bounds__(512, 2) void k2_main(const char* __restrict__ x, const char* __restrict__ wd, const char* __restrict__ wo,
                                                  char* __restrict__ ws, char* __restrict__ out) {
  const char* pc = (const char*)((size_t)getpc() & ~(size_t)255);
  const int w = __builtin_amdgcn_readfirstlane(threadIdx.x >> 6);
  const int xcd = blockIdx.x & 7, i = blockIdx.x >> 3;
  const int mt = xcd * 4 + (i & 3), cb = min((i >> 2) * 192, 1088);
  const int it0 = w < 4 ? threadIdx.x : threadIdx.x + 256;
  u32x4 xv[2];
  auto ldx = [&](int k) { const int it = it0 + 256 * k, row = it / 12; xv[k] = *(const u32x4*)(x + (size_t)(mt * 64 + row) * 1280 + cb + (it - row * 12) * 16); };
  auto fin = [&](int k) {
    const int it = it0 + 256 * k, row = it / 12, c = it - row * 12;
    f32x4 a = {bflo(xv[k][0]), bfhi(xv[k][0]), bflo(xv[k][1]), bfhi(xv[k][1])}, b = {bflo(xv[k][2]), bfhi(xv[k][2]), bflo(xv[k][3]), bfhi(xv[k][3])};
#pragma unroll
    for (int g = 0; g < 4; g++) {
      a += *(lds_f32x4)(size_t)(g * G_REG + row * 400 + c * 32);
      b += *(lds_f32x4)(size_t)(g * G_REG + row * 400 + c * 32 + 16);
    }
    *(u32x4*)(out + (size_t)(mt * 64 + row) * 1280 + cb + c * 16) = (u32x4){cvt2(a[0], a[1]), cvt2(a[2], a[3]), cvt2(b[0], b[1]), cvt2(b[2], b[3])};
  };
  if (w < 4) {
    __builtin_amdgcn_s_setprio(2);
    k2_attn(ws, w, xcd * 4 + (i >> 3), i & 7);
    if (i >= 28) return;
    ldx(0); ldx(1);
    BAR();
    fin(0); fin(1);
  } else {
    const int gw = w - 4;
    const char* kvb = ws + KV_OFF + (size_t)((xcd * 4 + (i >> 3)) * 64 + (gw >> 1) * 32 + 1) * KVT_B + (gw & 1) * 3072;
    set_m0(K2_LDS - 1024); asm volatile("s_nop 0");
    glds16o<0>((threadIdx.x & 63) * 16, pc + (i * 4 + gw) % (K2_CODE / 1024) * 1024);
    k2_kv(kvb, gw, 0); k2_kv(kvb, gw, 1);
    if (i >= 28) {
      for (int it = 0; it < 16; it++) {
        if (it < 15) WAIT_VM(3); else WAIT_VM(0);
        BAR();
        if (it < 14) k2_kv(kvb, gw, it + 2);
      }
      return;
    }
    k2_gemm(wd, wo, ws, kvb, gw, mt, cb, xcd, [&]() { ldx(0); asm volatile("" : "+v"(xv[0])); });
    WAIT_LGKM0();
    BAR();
    fin(0);
  }
}

extern "C" void kernel_launch(void* const* d_in, const int* in_sizes, int n_in, void* d_out, int out_size, void* d_ws, size_t ws_size,
                              hipStream_t stream) {
  const char* x = (const char*)d_in[0];
  const char* wqkv = (const char*)d_in[1];
  const char* wo = (const char*)d_in[2];
  const char* wg = (const char*)d_in[4];
  const char* wu = (const char*)d_in[5];
  const char* wd = (const char*)d_in[6];
  char* ws = (char*)d_ws;
  (void)hipFuncSetAttribute((const void*)k1_proj, hipFuncAttributeMaxDynamicSharedMemorySize, K1_LDS);
  (void)hipFuncSetAttribute((const void*)k2_main, hipFuncAttributeMaxDynamicSharedMemorySize, K2_LDS);
  k1_proj<<<256, 256, K1_LDS, stream>>>(x, wqkv, wg, wu, ws, wd, wo);
  k2_main<<<256, 512, K2_LDS, stream>>>(x, wd, wo, ws, (char*)d_out);
}
```
